```python
import torch

def _softmax(x, axis=-1):
    return torch.softmax(x, dim=axis)
def _gelu(x):
    return torch.nn.functional.gelu(x, approximate="tanh")
def _silu(x):
    return torch.nn.functional.silu(x)
def _sigmoid(x):
    return torch.sigmoid(x)
def _layernorm(x, axis=-1, eps=1e-5):
    m = x.mean(dim=axis, keepdim=True)
    v = x.var(dim=axis, keepdim=True, unbiased=False)
    return (x - m) / torch.sqrt(v + eps)
def _scatter_add(tgt, idx, src, axis=0):
    return tgt.clone().index_add_(axis, idx, src)
def _segment_sum(src, seg, n):
    out = torch.zeros((n,) + src.shape[1:], dtype=src.dtype)
    return out.index_add_(0, seg, src)
def _pad(x, pw):
    flat = tuple(v for pair in reversed(pw) for v in pair)
    return torch.nn.functional.pad(x, flat)

kernel_name = 'standalone__mpnn_edge_mlp__graph'

def setup_inputs():

    _w = (lambda a: (lambda t: t.to(torch.bfloat16) if t.is_floating_point() else t)(torch.as_tensor(a)))
    N, deg, D, De = 50000, 16, 64, max(16, 1)
    E = N * deg
    return {
        "x": _w((lambda *s: torch.randn(*s, dtype=torch.bfloat16) * 0.05)(N, D)),
        "e_attr": _w((lambda *s: torch.randn(*s, dtype=torch.bfloat16) * 0.4)(E, De)),
        "src": _w((lambda lo, hi, *s: torch.randint(lo, hi, s))(0, N, E).to(torch.int64)),
        "dst": _w((lambda lo, hi, *s: torch.randint(lo, hi, s))(0, N, E).to(torch.int64)),
        "w_msg": _w((lambda *s: torch.randn(*s, dtype=torch.bfloat16) * 0.4)(2 * D + De, D)),
        "w_upd": _w((lambda *s: torch.randn(*s, dtype=torch.bfloat16) * 0.05)(2 * D, D)),
    }

def reference(x, e_attr, src, dst, w_msg, w_upd):
    N, D = x.shape
    m_in = torch.concatenate([x[src], x[dst], e_attr], axis=-1)
    msg = _silu(m_in @ w_msg)
    agg = _segment_sum(msg, dst, N)
    return _silu(torch.concatenate([x, agg], axis=-1) @ w_upd)
```

```cpp
#include <hip/hip_runtime.h>
#include <hip/hip_bf16.h>
#include <stdint.h>
static __device__ inline float bf2f(uint16_t v){ return __uint_as_float(((uint32_t)v)<<16); }
static __device__ inline uint16_t f2bf(float f){ uint32_t u=__float_as_uint(f); u += 0x7fff + ((u>>16)&1); return (uint16_t)(u>>16); }
__global__ void k_zero(float* agg, int n){ int i=blockIdx.x*256+threadIdx.x; if(i<n) agg[i]=0.f; }
__global__ void k_msg(const uint16_t* x,const uint16_t* e,const int* src,const int* dst,const uint16_t* w,float* agg,int E){
  __shared__ float ws[144*64];
  for(int i=threadIdx.x;i<144*64;i+=256) ws[i]=bf2f(w[i]);
  __syncthreads();
  int f=threadIdx.x&63;
  for(int ed=blockIdx.x*4+(threadIdx.x>>6); ed<E; ed+=gridDim.x*4){
    int s=src[ed], d=dst[ed];
    float z=0.f;
    _Pragma("unroll 4") for(int k=0;k<64;k++) z+=bf2f(x[s*64+k])*ws[k*64+f];
    _Pragma("unroll 4") for(int k=0;k<64;k++) z+=bf2f(x[d*64+k])*ws[(64+k)*64+f];
    _Pragma("unroll 4") for(int k=0;k<16;k++) z+=bf2f(e[(size_t)ed*16+k])*ws[(128+k)*64+f];
    z=bf2f(f2bf(z));
    float m=z/(1.f+__expf(-z));
    m=bf2f(f2bf(m));
    atomicAdd(&agg[d*64+f],m);
  }
}
__global__ void k_upd(const uint16_t* x,const float* agg,const uint16_t* w,uint16_t* out,int N){
  __shared__ float ws[128*64];
  for(int i=threadIdx.x;i<128*64;i+=256) ws[i]=bf2f(w[i]);
  __syncthreads();
  int f=threadIdx.x&63;
  for(int n=blockIdx.x*4+(threadIdx.x>>6); n<N; n+=gridDim.x*4){
    float z=0.f;
    _Pragma("unroll 4") for(int k=0;k<64;k++) z+=bf2f(x[n*64+k])*ws[k*64+f];
    _Pragma("unroll 4") for(int k=0;k<64;k++) z+=bf2f(f2bf(agg[n*64+k]))*ws[(64+k)*64+f];
    z=bf2f(f2bf(z));
    float m=z/(1.f+__expf(-z));
    out[n*64+f]=f2bf(m);
  }
}
extern "C" void kernel_launch(void* const* d_in, const int* in_sizes, int n_in, void* d_out, int out_size, void* d_ws, size_t ws_size, hipStream_t stream){
  const uint16_t* x=(const uint16_t*)d_in[0]; const uint16_t* e=(const uint16_t*)d_in[1];
  const int* src=(const int*)d_in[2]; const int* dst=(const int*)d_in[3];
  const uint16_t* wm=(const uint16_t*)d_in[4]; const uint16_t* wu=(const uint16_t*)d_in[5];
  int N=in_sizes[0]/64, E=in_sizes[2];
  float* agg=(float*)d_ws;
  k_zero<<<(N*64+255)/256,256,0,stream>>>(agg,N*64);
  k_msg<<<2048,256,0,stream>>>(x,e,src,dst,wm,agg,E);
  k_upd<<<1024,256,0,stream>>>(x,agg,wu,(uint16_t*)d_out,N);
}
```

```cpp
#include <hip/hip_runtime.h>
#include <stdint.h>

typedef unsigned int u32;
typedef unsigned short u16;
typedef __attribute__((ext_vector_type(4))) unsigned int u32x4;
typedef __attribute__((ext_vector_type(2))) unsigned int u32x2;
typedef __attribute__((ext_vector_type(8))) __bf16 bf16x8;
typedef __attribute__((ext_vector_type(2))) __bf16 bf16x2;
typedef __attribute__((ext_vector_type(2))) float f32x2;
typedef __attribute__((ext_vector_type(16))) float f32x16;

constexpr int NN = 50000;
constexpr int NE = 800000;
constexpr int CH = 3125;
constexpr int CAP = 512;

constexpr size_t WS_ES = 0;
constexpr size_t WS_MF = 26u << 20;
constexpr size_t WS_MO = 35u << 20;
constexpr size_t WS_U1 = 39u << 20;
constexpr size_t WS_PK = 46u << 20;

constexpr float C_NLOG2E = -1.4426950408889634f;
constexpr float C_NLN2 = -0.6931471805599453f;

static __device__ __forceinline__ float bf2f(u32 v) { return __uint_as_float(v << 16); }
static __device__ __forceinline__ u32 pk2(float a, float b) {
  f32x2 v = {a, b};
  bf16x2 r = __builtin_convertvector(v, bf16x2);
  return __builtin_bit_cast(u32, r);
}
static __device__ __forceinline__ f32x16 mfma(u32x4 a, u32x4 b, f32x16 c) {
  return __builtin_amdgcn_mfma_f32_32x32x16_bf16(__builtin_bit_cast(bf16x8, a), __builtin_bit_cast(bf16x8, b), c, 0, 0, 0);
}
static __device__ __forceinline__ int win_start(int v) { return (v * 3125) >> 7; }
static __device__ __forceinline__ u32 win_of(u32 n) { return (n * 128u + 127u) / 3125u; }
static __device__ __forceinline__ int swap23(int r) { return (r & ~12) | ((r & 4) << 1) | ((r & 8) >> 1); }
static __device__ __forceinline__ int phi(int T, int r) { return 32 * ((r >> 2) & 1) + 16 * T + (r & 3) + 4 * (r >> 3); }

__global__ __launch_bounds__(1024) void mpnn_k1(const u16* __restrict__ x, const u16* __restrict__ e,
                                                const int* __restrict__ src, const int* __restrict__ dst,
                                                const u16* __restrict__ wm, const u16* __restrict__ wu,
                                                char* __restrict__ ws) {
  __shared__ __attribute__((aligned(16))) u32 lds[8192 + 2048 + 256 + 256 + 1568];
  u32* mimg = lds;
  u32* wfr = lds + 8192;
  u32* hist = lds + 10240;
  u32* seg = lds + 10496;
  u16* pos = (u16*)(lds + 10752);

  const int c = blockIdx.x, t = threadIdx.x, lane = t & 63, wv = t >> 6;
  const int ebase = c * CH;

  int dv[4], sv[4];
#pragma unroll
  for (int j = 0; j < 4; ++j) {
    int i = t + 1024 * j;
    if (j < 3 || i < CH) { dv[j] = dst[ebase + i]; sv[j] = src[ebase + i]; } else { dv[j] = 0; sv[j] = 0; }
  }
  u32 w1[8];
  if (t < 512) {
    const int f = t >> 6, T = f >> 2, s = f & 3, r = lane & 31, h = lane >> 5;
    const u16* p = wm + (16 * s + 8 * h) * 64 + phi(T, r);
#pragma unroll
    for (int j = 0; j < 8; ++j) w1[j] = p[j * 64];
  }
  const int tile = c + 256 * wv;
  const bool has_tile = (wv < 7) && (tile < 1563);
  u32x4 xb[4];
  int tnode = tile * 32 + (lane & 31);
  if (has_tile) {
    const char* xp = (const char*)x + (size_t)min(tnode, NN - 1) * 128 + (lane >> 5) * 16;
#pragma unroll
    for (int s = 0; s < 4; ++s) xb[s] = *(const u32x4*)(xp + 32 * s);
  }
  u32x4 ev[7];
  const char* ep = (const char*)e + (size_t)ebase * 32;
#pragma unroll
  for (int j = 0; j < 7; ++j) {
    int pc = t + 1024 * j;
    if (j < 6 || pc < 2 * CH) ev[j] = *(const u32x4*)(ep + (size_t)pc * 16);
  }
  if (t < 256) hist[t] = 0;
  __syncthreads();

  u32 bk[4], rk[4], wd[4];
#pragma unroll
  for (int j = 0; j < 4; ++j) {
    int i = t + 1024 * j;
    u32 v = win_of((u32)dv[j]);
    bk[j] = v >> 3;
    wd[j] = ((v & 7u) << 5) | (u32)(dv[j] - win_start((int)v));
    if (j < 3 || i < CH) rk[j] = atomicAdd(&hist[bk[j]], 1u);
  }
  if (t < 512) {
    u32x4 p = {w1[0] | (w1[1] << 16), w1[2] | (w1[3] << 16), w1[4] | (w1[5] << 16), w1[6] | (w1[7] << 16)};
    *(u32x4*)(wfr + t * 4) = p;
  }
  __syncthreads();

  if (wv == 15) {
    u32x4 hv = *(const u32x4*)(hist + 4 * lane);
    u32 tot = hv[0] + hv[1] + hv[2] + hv[3];
    u32 inc = tot;
#pragma unroll
    for (int o = 1; o < 64; o <<= 1) {
      u32 n = __shfl_up(inc, o);
      if (lane >= o) inc += n;
    }
    u32 b0 = inc - tot;
    u32x4 sg = {b0, b0 + hv[0], b0 + hv[0] + hv[1], b0 + hv[0] + hv[1] + hv[2]};
    *(u32x4*)(seg + 4 * lane) = sg;
#pragma unroll
    for (int k = 0; k < 4; ++k) mimg[(4 * lane + k) * 32] = hv[k] | (sg[k] << 12);
  } else if (has_tile) {
    f32x16 a0 = {0}, a1 = {0};
#pragma unroll
    for (int s = 0; s < 4; ++s) {
      a0 = mfma(*(const u32x4*)(wfr + (s * 64 + lane) * 4), xb[s], a0);
      a1 = mfma(*(const u32x4*)(wfr + ((4 + s) * 64 + lane) * 4), xb[s], a1);
    }
    if (tnode < NN) {
      char* up = ws + WS_U1 + (size_t)tnode * 128 + (lane >> 5) * 64;
#pragma unroll
      for (int q = 0; q < 2; ++q) {
        u32x4 o0, o1;
#pragma unroll
        for (int k = 0; k < 4; ++k) {
          o0[k] = pk2(a0[8 * q + 2 * k] * C_NLOG2E, a0[8 * q + 2 * k + 1] * C_NLOG2E);
          o1[k] = pk2(a1[8 * q + 2 * k] * C_NLOG2E, a1[8 * q + 2 * k + 1] * C_NLOG2E);
        }
        *(u32x4*)(up + 16 * q) = o0;
        *(u32x4*)(up + 32 + 16 * q) = o1;
      }
    }
  } else if (wv == 6 && c < 27 + 28) {
    const int f = c - 27, r = lane & 31, h = lane >> 5;
    u32 val[8];
    if (f < 8) {
      const int s = f >> 1, half = f & 1;
#pragma unroll
      for (int j = 0; j < 8; ++j) val[j] = wm[(64 + 16 * s + 8 * h + j) * 64 + 32 * half + r];
    } else if (f < 12) {
      const int half = f & 1, lo = (f - 8) >> 1;
#pragma unroll
      for (int j = 0; j < 8; ++j) {
        float w = bf2f(wm[(128 + 8 * h + j) * 64 + 32 * half + r]) * C_NLOG2E;
        u32 hi = pk2(w, 0.f) & 0xffffu;
        val[j] = lo ? (pk2(w - bf2f(hi), 0.f) & 0xffffu) : hi;
      }
    } else {
      const int g = f - 12, T = g >> 3, ks = g & 7;
#pragma unroll
      for (int j = 0; j < 8; ++j) {
        int k = ks < 4 ? 16 * ks + 8 * h + j : 64 + 16 * (ks - 4) + 8 * (j >> 2) + 4 * h + (j & 3);
        val[j] = wu[k * 64 + phi(T, r)];
      }
    }
    u32x4 p = {val[0] | (val[1] << 16), val[2] | (val[3] << 16), val[4] | (val[5] << 16), val[6] | (val[7] << 16)};
    *(u32x4*)(ws + WS_PK + (size_t)(f * 64 + lane) * 16) = p;
  }
  __syncthreads();

#pragma unroll
  for (int j = 0; j < 4; ++j) {
    int i = t + 1024 * j;
    if (j < 3 || i < CH) {
      u32 p = seg[bk[j]] + rk[j];
      pos[i] = (u16)p;
      u32 rec = ((u32)sv[j] << 8) | wd[j];
      if (rk[j] < 31) mimg[bk[j] * 32 + 1 + rk[j]] = rec;
      else *(u32*)(ws + WS_MO + (size_t)(ebase + p) * 4) = rec;
    }
  }
  __syncthreads();

#pragma unroll
  for (int j = 0; j < 2; ++j) {
    int pc = t + 1024 * j;
    u32x4 m = *(const u32x4*)(mimg + pc * 4);
    *(u32x4*)(ws + WS_MF + ((size_t)(pc >> 3) * 256 + c) * 128 + (pc & 7) * 16) = m;
  }
  char* es = ws + WS_ES + (size_t)ebase * 32;
#pragma unroll
  for (int j = 0; j < 7; ++j) {
    int pc = t + 1024 * j;
    if (j < 6 || pc < 2 * CH) {
      u32 p = pos[pc >> 1];
      *(u32x4*)(es + (size_t)p * 32 + (pc & 1) * 16) = ev[j];
    }
  }
}

constexpr int L_STG = 0;
constexpr int L_LS = 81920;
constexpr int L_LQ = L_LS + 8192;
constexpr int L_MK = L_LQ + 16384;
constexpr int L_ST = L_MK + 16384;
constexpr int L_TT = L_ST + 4096;
constexpr int L_CN = L_TT + 256;
constexpr int L_WU = L_CN + 64;
constexpr int L_END = L_WU + 16384;

static __device__ __forceinline__ void dma16(u32 voff, const char* sbase, u32 ldsaddr) {
  asm volatile("s_mov_b32 m0, %1\n\ts_nop 0\n\tglobal_load_lds_dwordx4 %0, %2" ::"v"(voff), "s"(ldsaddr), "s"(sbase) : "memory");
}

__global__ __launch_bounds__(512, 2) void mpnn_k2(const u16* __restrict__ x, const char* __restrict__ ws,
                                                  u16* __restrict__ out) {
  __shared__ __attribute__((aligned(16))) char lds[L_END];
  const int t = threadIdx.x, lane = t & 63;
  const int w = __builtin_amdgcn_readfirstlane(t >> 6);
  const int b = (blockIdx.x & 7) * 32 + (blockIdx.x >> 3);
  const int v = 8 * b + w;
  const int ws0 = win_start(v), nwin = win_start(v + 1) - ws0;
  const int r = lane & 31, h = lane >> 5;
  const int pr = swap23(r);

  const char* mf = ws + WS_MF + (size_t)b * 256 * 128;
  u32x4 mr[4];
#pragma unroll
  for (int k = 0; k < 4; ++k) mr[k] = *(const u32x4*)(mf + (size_t)(t + 512 * k) * 16);
  const int node = ws0 + pr;
  u32x4 xf[4];
  {
    const char* xp = (const char*)x + (size_t)min(node, NN - 1) * 128 + h * 16;
#pragma unroll
    for (int s = 0; s < 4; ++s) xf[s] = *(const u32x4*)(xp + 32 * s);
  }
  const char* pk = ws + WS_PK + lane * 16;
  u32x4 w2[8];
#pragma unroll
  for (int f = 0; f < 8; ++f) w2[f] = *(const u32x4*)(pk + f * 1024);
  u32x4 w3[4];
#pragma unroll
  for (int f = 0; f < 4; ++f) w3[f] = *(const u32x4*)(pk + (8 + f) * 1024);
  u32x4 wut[2];
#pragma unroll
  for (int k = 0; k < 2; ++k) wut[k] = *(const u32x4*)(ws + WS_PK + 12 * 1024 + (size_t)(t + 512 * k) * 16);

  const u32x4 z4 = {0, 0, 0, 0};
#pragma unroll
  for (int k = 0; k < 5; ++k) *(u32x4*)(lds + L_LS + (t + 512 * k) * 16) = z4;
  if (t < 256) {
    u32x4 s;
#pragma unroll
    for (int i = 0; i < 4; ++i) s[i] = (((t >> (2 * i)) & 1) ? 0x3F80u : 0u) | (((t >> (2 * i + 1)) & 1) ? 0x3F800000u : 0u);
    *(u32x4*)(lds + L_ST + t * 16) = s;
  } else if (t < 256 + 9) {
    const int k = t - 256;
    u32x4 s;
#pragma unroll
    for (int i = 0; i < 4; ++i) s[i] = (k == 2 * i ? 0x3F80u : 0u) | (k == 2 * i + 1 ? 0x3F800000u : 0u);
    *(u32x4*)(lds + L_TT + k * 16) = s;
  } else if (t >= 320 && t < 320 + 16) {
    *(u32*)(lds + L_CN + (t - 320) * 4) = 0;
  }
  __syncthreads();

  u32* cnt = (u32*)(lds + L_CN);
  u16* LS = (u16*)(lds + L_LS);
  u32* LQ = (u32*)(lds + L_LQ);
  u32* MK = (u32*)(lds + L_MK);

  {
    const int sub = t & 7;
    bool bad = false;
#pragma unroll
    for (int k = 0; k < 4; ++k) {
      const int c = (t >> 3) + 64 * k;
      u32 hdr = (u32)__builtin_amdgcn_ds_swizzle((int)mr[k][0], 0x0018);
      const int count = hdr & 0xfff;
      const u32 qb = (u32)(c * CH) + (hdr >> 12) - 1;
      bad |= count > 31;
#pragma unroll
      for (int j = 0; j < 4; ++j) {
        const int slot = 4 * sub + j;
        if (slot >= 1 && slot <= count) {
          const u32 rec = mr[k][j];
          const u32 ww = (rec >> 5) & 7u, d = rec & 31u;
          const u32 p = atomicAdd(&cnt[ww], 1u);
          if (p < CAP) {
            LS[ww * CAP + p] = (u16)(rec >> 8);
            LQ[ww * CAP + p] = ((qb + slot) << 5) | d;
            atomicOr(&MK[(ww * 16 + (p >> 5)) * 32 + d], 1u << (p & 31));
          } else {
            bad = true;
          }
        }
      }
    }
    if (bad) cnt[8] = 1;
  }
#pragma unroll
  for (int k = 0; k < 2; ++k) *(u32x4*)(lds + L_WU + (t + 512 * k) * 16) = wut[k];

  u32x4 u2[2][2];
  {
    f32x16 a0 = {0}, a1 = {0};
#pragma unroll
    for (int s = 0; s < 4; ++s) {
      a0 = mfma(xf[s], w2[2 * s], a0);
      a1 = mfma(xf[s], w2[2 * s + 1], a1);
    }
#pragma unroll
    for (int s = 0; s < 2; ++s)
#pragma unroll
      for (int k = 0; k < 4; ++k) {
        u2[s][0][k] = pk2(a0[8 * s + 2 * k] * C_NLOG2E, a0[8 * s + 2 * k + 1] * C_NLOG2E);
        u2[s][1][k] = pk2(a1[8 * s + 2 * k] * C_NLOG2E, a1[8 * s + 2 * k + 1] * C_NLOG2E);
      }
  }
  u32x4 ilo, ihi;
#pragma unroll
  for (int i = 0; i < 4; ++i) {
    ilo[i] = (r == 8 * h + 2 * i ? 0x3F80u : 0u) | (r == 8 * h + 2 * i + 1 ? 0x3F800000u : 0u);
    ihi[i] = (r == 16 + 8 * h + 2 * i ? 0x3F80u : 0u) | (r == 16 + 8 * h + 2 * i + 1 ? 0x3F800000u : 0u);
  }
  __syncthreads();

  const bool slow = __builtin_amdgcn_readfirstlane(cnt[8]) != 0;
  int sc = 0, si = 0;

  const u32 lbase = (u32)(size_t)lds;
  const u32 stg = L_STG + w * 10240;
  const int o8 = lane >> 3;
  u32 xsw[4];
#pragma unroll
  for (int i = 0; i < 4; ++i) xsw[i] = (u32)(((lane & 7) ^ (2 * i + (o8 >> 2))) << 4);
  const u32 esw = (u32)((((lane & 1) ^ ((lane >> 4) & 1))) << 4);
  const int ls_off = L_LS + w * (CAP * 2) + o8 * 8;
  const int lq_e = L_LQ + w * (CAP * 4) + swap23(lane >> 1) * 4;
  const int lq_d = L_LQ + w * (CAP * 4) + pr * 4;
  const int mk_off = L_MK + w * 2048 + pr * 4;
  int a_off;
  {
    const int i = pr & 3, o = pr >> 2;
    a_off = stg + (8 * i + o) * 128 + ((h ^ (2 * i + (o >> 2))) << 4);
  }
  const int e_off = stg + 4096 + r * 32 + ((h ^ ((r >> 3) & 1)) << 4);
  const char* u1base = ws + WS_U1;
  const char* esbase = ws + WS_ES;

  f32x16 ag0 = {0}, ag1 = {0};

  for (;;) {
    if (slow) {
      __syncthreads();
#pragma unroll
      for (int k = 0; k < 5; ++k) *(u32x4*)(lds + L_LS + (t + 512 * k) * 16) = z4;
      if (t < 8) cnt[t] = 0;
      __syncthreads();
      const u32 hdr = *(const u32*)(mf + (size_t)sc * 128);
      const int count = hdr & 0xfff;
      const int i = si + t;
      if (i < count) {
        const u32 q = (u32)(sc * CH) + (hdr >> 12) + i;
        const u32 rec = i < 31 ? *(const u32*)(mf + (size_t)sc * 128 + 4 + i * 4) : *(const u32*)(ws + WS_MO + (size_t)q * 4);
        const u32 ww = (rec >> 5) & 7u, d = rec & 31u;
        const u32 p = atomicAdd(&cnt[ww], 1u);
        LS[ww * CAP + p] = (u16)(rec >> 8);
        LQ[ww * CAP + p] = (q << 5) | d;
        atomicOr(&MK[(ww * 16 + (p >> 5)) * 32 + d], 1u << (p & 31));
      }
      si += CAP;
      if (si >= __builtin_amdgcn_readfirstlane(count)) { si = 0; ++sc; }
      __syncthreads();
    }
    const int ng = (__builtin_amdgcn_readfirstlane(cnt[w]) + 31) >> 5;

    auto issue = [&](int g) {
      const u32x2 sp = *(const u32x2*)(lds + ls_off + g * 64);
      const u32 qw = *(const u32*)(lds + lq_e + g * 128);
      const u32 dstb = lbase + stg + (g & 1) * 5120;
      dma16(((sp[0] & 0xffffu) << 7) | xsw[0], u1base, dstb);
      dma16(((sp[0] >> 16) << 7) | xsw[1], u1base, dstb + 1024);
      dma16(((sp[1] & 0xffffu) << 7) | xsw[2], u1base, dstb + 2048);
      dma16(((sp[1] >> 16) << 7) | xsw[3], u1base, dstb + 3072);
      dma16((qw & ~31u) | esw, esbase, dstb + 4096);
    };
    if (ng > 0) issue(0);
    if (ng > 1) issue(1);
    for (int g = 0; g < ng; ++g) {
      if (g + 1 < ng) asm volatile("s_waitcnt vmcnt(5)" ::: "memory");
      else asm volatile("s_waitcnt vmcnt(0)" ::: "memory");
      const int bo = (g & 1) * 5120;
      u32x4 au[4];
#pragma unroll
      for (int s = 0; s < 4; ++s) au[s] = *(const u32x4*)(lds + ((a_off + bo) ^ (s << 5)));
      const u32x4 ae = *(const u32x4*)(lds + e_off + bo);
      const u32 dw = *(const u32*)(lds + lq_d + g * 128) & 31u;
      const u32 mk = *(const u32*)(lds + mk_off + g * 128);
      u32x4 pf[2], sf[2];
#pragma unroll
      for (int s = 0; s < 2; ++s) {
        const u32 idx = min(dw - (u32)(16 * s + 8 * h), 8u);
        pf[s] = *(const u32x4*)(lds + L_TT + idx * 16);
        sf[s] = *(const u32x4*)(lds + L_ST + ((mk >> (16 * s + 8 * h)) & 255u) * 16);
      }
      asm volatile("s_waitcnt lgkmcnt(0)" ::: "memory");
      if (g + 2 < ng) issue(g + 2);

      f32x16 z0 = {0}, z1 = {0};
      z0 = mfma(ae, w3[0], z0);
      z1 = mfma(ae, w3[1], z1);
      z0 = mfma(ae, w3[2], z0);
      z1 = mfma(ae, w3[3], z1);
      z0 = mfma(pf[0], u2[0][0], z0);
      z1 = mfma(pf[0], u2[0][1], z1);
      z0 = mfma(pf[1], u2[1][0], z0);
      z1 = mfma(pf[1], u2[1][1], z1);
      z0 = mfma(au[0], ilo, z0);
      z1 = mfma(au[2], ilo, z1);
      z0 = mfma(au[1], ihi, z0);
      z1 = mfma(au[3], ihi, z1);
      u32x4 m0[2], m1[2];
#pragma unroll
      for (int s = 0; s < 2; ++s)
#pragma unroll
        for (int k = 0; k < 4; ++k) {
          float ua = z0[8 * s + 2 * k], ub = z0[8 * s + 2 * k + 1];
          float ra = __builtin_amdgcn_rcpf(1.f + __builtin_amdgcn_exp2f(ua));
          float rb = __builtin_amdgcn_rcpf(1.f + __builtin_amdgcn_exp2f(ub));
          m0[s][k] = pk2(ua * ra, ub * rb);
        }
      ag0 = mfma(m0[0], sf[0], ag0);
      ag0 = mfma(m0[1], sf[1], ag0);
#pragma unroll
      for (int s = 0; s < 2; ++s)
#pragma unroll
        for (int k = 0; k < 4; ++k) {
          float ua = z1[8 * s + 2 * k], ub = z1[8 * s + 2 * k + 1];
          float ra = __builtin_amdgcn_rcpf(1.f + __builtin_amdgcn_exp2f(ua));
          float rb = __builtin_amdgcn_rcpf(1.f + __builtin_amdgcn_exp2f(ub));
          m1[s][k] = pk2(ua * ra, ub * rb);
        }
      ag1 = mfma(m1[0], sf[0], ag1);
      ag1 = mfma(m1[1], sf[1], ag1);
    }
    if (!slow || sc >= 256) break;
  }

  u32x4 gf[4];
#pragma unroll
  for (int s = 0; s < 2; ++s)
#pragma unroll
    for (int k = 0; k < 4; ++k) {
      gf[s][k] = pk2(ag0[8 * s + 2 * k] * C_NLN2, ag0[8 * s + 2 * k + 1] * C_NLN2);
      gf[2 + s][k] = pk2(ag1[8 * s + 2 * k] * C_NLN2, ag1[8 * s + 2 * k + 1] * C_NLN2);
    }
  f32x16 o0 = {0}, o1 = {0};
#pragma unroll
  for (int ks = 0; ks < 4; ++ks) {
    o0 = mfma(*(const u32x4*)(lds + L_WU + ks * 1024 + lane * 16), xf[ks], o0);
    o1 = mfma(*(const u32x4*)(lds + L_WU + (8 + ks) * 1024 + lane * 16), xf[ks], o1);
  }
#pragma unroll
  for (int ks = 0; ks < 4; ++ks) {
    o0 = mfma(*(const u32x4*)(lds + L_WU + (4 + ks) * 1024 + lane * 16), gf[ks], o0);
    o1 = mfma(*(const u32x4*)(lds + L_WU + (12 + ks) * 1024 + lane * 16), gf[ks], o1);
  }
  if (pr < nwin) {
    char* op = (char*)out + (size_t)node * 128 + h * 64;
#pragma unroll
    for (int q = 0; q < 2; ++q) {
      u32x4 s0, s1;
#pragma unroll
      for (int k = 0; k < 4; ++k) {
        float a = o0[8 * q + 2 * k], bb = o0[8 * q + 2 * k + 1];
        s0[k] = pk2(a * __builtin_amdgcn_rcpf(1.f + __builtin_amdgcn_exp2f(a * C_NLOG2E)),
                    bb * __builtin_amdgcn_rcpf(1.f + __builtin_amdgcn_exp2f(bb * C_NLOG2E)));
        a = o1[8 * q + 2 * k]; bb = o1[8 * q + 2 * k + 1];
        s1[k] = pk2(a * __builtin_amdgcn_rcpf(1.f + __builtin_amdgcn_exp2f(a * C_NLOG2E)),
                    bb * __builtin_amdgcn_rcpf(1.f + __builtin_amdgcn_exp2f(bb * C_NLOG2E)));
      }
      *(u32x4*)(op + 16 * q) = s0;
      *(u32x4*)(op + 32 + 16 * q) = s1;
    }
  }
}

extern "C" void kernel_launch(void* const* d_in, const int* in_sizes, int n_in, void* d_out, int out_size,
                              void* d_ws, size_t ws_size, hipStream_t stream) {
  const u16* x = (const u16*)d_in[0];
  const u16* e = (const u16*)d_in[1];
  const int* src = (const int*)d_in[2];
  const int* dst = (const int*)d_in[3];
  const u16* wm = (const u16*)d_in[4];
  const u16* wu = (const u16*)d_in[5];
  mpnn_k1<<<256, 1024, 0, stream>>>(x, e, src, dst, wm, wu, (char*)d_ws);
  mpnn_k2<<<256, 512, 0, stream>>>(x, (const char*)d_ws, (u16*)d_out);
}
```

```cpp
#include <hip/hip_runtime.h>
#include <stdint.h>

typedef unsigned int u32;
typedef unsigned short u16;
typedef __attribute__((ext_vector_type(4))) unsigned int u32x4;
typedef __attribute__((ext_vector_type(2))) unsigned int u32x2;
typedef __attribute__((ext_vector_type(8))) unsigned int u32x8;
typedef __attribute__((ext_vector_type(16))) unsigned int u32x16;
typedef __attribute__((ext_vector_type(32))) unsigned int u32x32;
typedef __attribute__((ext_vector_type(8))) __bf16 bf16x8;
typedef __attribute__((ext_vector_type(2))) __bf16 bf16x2;
typedef __attribute__((ext_vector_type(2))) float f32x2;
typedef __attribute__((ext_vector_type(16))) float f32x16;

constexpr int NN = 50000;
constexpr int NE = 800000;
constexpr int CH = 3125;
constexpr int CAP = 512;

constexpr size_t WS_MA = 0;
constexpr size_t WS_MB = 8u << 20;
constexpr size_t WS_MO = 16u << 20;
constexpr size_t WS_PK = 24u << 20;
constexpr int FR_W2 = 0, FR_W3 = 8, FR_W1 = 10, FR_WU = 18, NFRAG = 34;

constexpr float C_NLOG2E = -1.4426950408889634f;
constexpr float C_NLN2 = -0.6931471805599453f;

static __device__ __forceinline__ float bf2f(u32 v) { return __uint_as_float(v << 16); }
static __device__ __forceinline__ u32 pk2(float a, float b) {
  f32x2 v = {a, b};
  bf16x2 r = __builtin_convertvector(v, bf16x2);
  return __builtin_bit_cast(u32, r);
}
static __device__ __forceinline__ f32x16 mfma(u32x4 a, u32x4 b, f32x16 c) {
  return __builtin_amdgcn_mfma_f32_32x32x16_bf16(__builtin_bit_cast(bf16x8, a), __builtin_bit_cast(bf16x8, b), c, 0, 0, 0);
}
static __device__ __forceinline__ int win_start(int v) { return (v * 3125) >> 7; }
static __device__ __forceinline__ u32 win_of(u32 n) { return (n * 128u + 127u) / 3125u; }
static __device__ __forceinline__ int swap23(int r) { return (r & ~12) | ((r & 4) << 1) | ((r & 8) >> 1); }
static __device__ __forceinline__ int phi(int T, int r) { return 32 * ((r >> 2) & 1) + 16 * T + (r & 3) + 4 * (r >> 3); }

#define LDS_BAR() asm volatile("s_waitcnt lgkmcnt(0)\n\ts_barrier" ::: "memory")
#define WAIT_VM(n) asm volatile("s_waitcnt vmcnt(" #n ")" ::: "memory")
#define WAIT_VMN(n) asm volatile("s_waitcnt vmcnt(%0)" ::"n"(n) : "memory")

static __device__ __forceinline__ void dma16(u32 voff, const char* sbase, u32 ldsaddr) {
  asm volatile("s_mov_b32 m0, %1\n\ts_nop 0\n\tglobal_load_lds_dwordx4 %0, %2" ::"v"(voff), "s"(ldsaddr), "s"(sbase) : "memory");
}


#define CLOB "v104", "v105", "v106", "v107", "v108", "v109", "v110", "v111", "v112", "v113", "v114", "v115", "v116", "v117", "v118", "v119", "v120", "v121", "v122", "v123", "v124", "v125", "v126", "v127", "v128", "v129", "v130", "v131", "v132", "v133", "v134", "v135", "v136", "v137", "v138", "v139", "v140", "v141", "v142", "v143", "v144", "v145", "v146", "v147", "v148", "v149", "v150", "v151", "v152", "v153", "v154", "v155", "v156", "v157", "v158", "v159", "v160", "v161", "v162", "v163", "v164", "v165", "v166", "v167", "v168", "v169", "v170", "v171", "v172", "v173", "v174", "v175", "v176", "v177", "v178", "v179", "v180", "v181", "v182", "v183", "v184", "v185", "v186", "v187", "v188", "v189", "v190", "v191", "v192", "v193", "v194", "v195", "v196", "v197", "v198", "v199", "v200", "v201", "v202", "v203", "v204", "v205", "v206", "v207", "v208", "v209", "v210", "v211", "v212", "v213"
#define K2_LOOP_ASM \
  "s_waitcnt lgkmcnt(0)\n" \
  "s_cmp_lt_i32 %[rem], 1\n" \
  "s_cbranch_scc1 .Lend_%=\n" \
  "ds_read_b128 v[204:207], v93 offset:0\n" \
  "ds_read_b32 v208, v94 offset:0\n" \
  "s_waitcnt lgkmcnt(0)\n" \
  "v_and_or_b32 v204, v204, %[cmx], v88\n" \
  "v_and_or_b32 v205, v205, %[cmx], v89\n" \
  "v_and_or_b32 v206, v206, %[cmx], v90\n" \
  "v_and_or_b32 v207, v207, %[cmx], v91\n" \
  "v_or_b32_e32 v208, v208, v92\n" \
  "s_mov_b32 m0, %[mb0]\n" \
  "s_nop 0\n" \
  "global_load_lds_dwordx4 v204, %[xb0] offset:0\n" \
  "global_load_lds_dwordx4 v205, %[xb1] offset:1024\n" \
  "global_load_lds_dwordx4 v206, %[xb2] offset:2048\n" \
  "global_load_lds_dwordx4 v207, %[xb3] offset:3072\n" \
  "s_mov_b32 m0, %[me0]\n" \
  "s_nop 0\n" \
  "global_load_lds_dwordx4 v208, %[eb] nt\n" \
  "s_cmp_gt_i32 %[rem], 1\n" \
  "s_cbranch_scc0 .Lp1_%=\n" \
  "ds_read_b128 v[204:207], v93 offset:128\n" \
  "ds_read_b32 v208, v94 offset:128\n" \
  "s_waitcnt lgkmcnt(0)\n" \
  "v_and_or_b32 v204, v204, %[cmx], v88\n" \
  "v_and_or_b32 v205, v205, %[cmx], v89\n" \
  "v_and_or_b32 v206, v206, %[cmx], v90\n" \
  "v_and_or_b32 v207, v207, %[cmx], v91\n" \
  "v_or_b32_e32 v208, v208, v92\n" \
  "s_mov_b32 m0, %[mb1]\n" \
  "s_nop 0\n" \
  "global_load_lds_dwordx4 v204, %[xb0] offset:0\n" \
  "global_load_lds_dwordx4 v205, %[xb1] offset:1024\n" \
  "global_load_lds_dwordx4 v206, %[xb2] offset:2048\n" \
  "global_load_lds_dwordx4 v207, %[xb3] offset:3072\n" \
  "s_mov_b32 m0, %[me1]\n" \
  "s_nop 0\n" \
  "global_load_lds_dwordx4 v208, %[eb] nt\n" \
  "s_waitcnt vmcnt(5)\n" \
  "s_branch .Lp2_%=\n" \
  ".Lp1_%=:\n" \
  "s_waitcnt vmcnt(0)\n" \
  ".Lp2_%=:\n" \
  "ds_read_b32 v209, v95\n" \
  "ds_read_b128 v[152:155], v101\n" \
  "ds_read_b128 v[136:139], v97\n" \
  "ds_read_b128 v[140:143], v98\n" \
  "ds_read_b128 v[144:147], v99\n" \
  "ds_read_b128 v[148:151], v100\n" \
  "ds_read_b128 v[204:207], v93 offset:256\n" \
  "ds_read_b32 v208, v94 offset:256\n" \
  "s_waitcnt lgkmcnt(7)\n" \
  "v_and_b32_e32 v209, 0x7c, v209\n" \
  "v_lshl_add_u32 v211, v209, 2, v102\n" \
  "ds_read_b128 v[156:159], v211 offset:256\n" \
  "ds_read_b128 v[160:163], v211\n" \
  "v_mov_b32_e32 v180, 0\n" \
  "v_mov_b32_e32 v164, 0\n" \
  "v_mov_b32_e32 v181, 0\n" \
  "v_mov_b32_e32 v165, 0\n" \
  "v_mov_b32_e32 v182, 0\n" \
  "v_mov_b32_e32 v166, 0\n" \
  "v_mov_b32_e32 v183, 0\n" \
  "v_mov_b32_e32 v167, 0\n" \
  "v_mov_b32_e32 v184, 0\n" \
  "v_mov_b32_e32 v168, 0\n" \
  "v_mov_b32_e32 v185, 0\n" \
  "v_mov_b32_e32 v169, 0\n" \
  "v_mov_b32_e32 v186, 0\n" \
  "v_mov_b32_e32 v170, 0\n" \
  "v_mov_b32_e32 v187, 0\n" \
  "v_mov_b32_e32 v171, 0\n" \
  "s_waitcnt lgkmcnt(0)\n" \
  "v_and_or_b32 v204, v204, %[cmx], v88\n" \
  "v_and_or_b32 v205, v205, %[cmx], v89\n" \
  "v_and_or_b32 v206, v206, %[cmx], v90\n" \
  "v_and_or_b32 v207, v207, %[cmx], v91\n" \
  "v_or_b32_e32 v208, v208, v92\n" \
  "s_cmp_gt_i32 %[rem], 2\n" \
  "s_cbranch_scc0 .Lp3_%=\n" \
  "s_mov_b32 m0, %[mb0]\n" \
  "s_nop 0\n" \
  "global_load_lds_dwordx4 v204, %[xb0] offset:0\n" \
  "global_load_lds_dwordx4 v205, %[xb1] offset:1024\n" \
  "global_load_lds_dwordx4 v206, %[xb2] offset:2048\n" \
  "global_load_lds_dwordx4 v207, %[xb3] offset:3072\n" \
  "s_mov_b32 m0, %[me0]\n" \
  "s_nop 0\n" \
  "global_load_lds_dwordx4 v208, %[eb] nt\n" \
  ".Lp3_%=:\n" \
  "v_mfma_f32_32x32x16_bf16 v[104:119], v[152:155], v[80:83], 0\n" \
  "v_mfma_f32_32x32x16_bf16 v[104:119], v[156:159], v[64:67], v[104:119]\n" \
  "v_mfma_f32_32x32x16_bf16 v[104:119], v[160:163], v[72:75], v[104:119]\n" \
  "v_mfma_f32_32x32x16_bf16 v[104:119], v[136:139], v[32:35], v[104:119]\n" \
  "v_mfma_f32_32x32x16_bf16 v[104:119], v[140:143], v[40:43], v[104:119]\n" \
  "v_mfma_f32_32x32x16_bf16 v[104:119], v[144:147], v[48:51], v[104:119]\n" \
  "v_mfma_f32_32x32x16_bf16 v[104:119], v[148:151], v[56:59], v[104:119]\n" \
  "s_nop 7\n" \
  "s_nop 7\n" \
  ".Lloop_%=:\n" \
  "s_sub_i32 %[ps], %[rem], %[pb]\n" \
  "v_mov_b32_e32 %[tw], %[ps]\n" \
  "ds_write_b32 %[ao], %[tw]\n" \
  "ds_read_b32 %[tr], %[ap]\n" \
  "ds_read_b32 v210, v96 offset:0\n" \
  "ds_read_b128 v[204:207], v93 offset:384\n" \
  "ds_read_b32 v208, v94 offset:384\n" \
  "v_exp_f32_e32 v188, v104\n" \
  "v_mfma_f32_32x32x16_bf16 v[16:31], v[180:183], v[164:167], v[16:31]\n" \
  "v_exp_f32_e32 v189, v105\n" \
  "v_exp_f32_e32 v190, v106\n" \
  "v_exp_f32_e32 v191, v107\n" \
  "v_exp_f32_e32 v192, v108\n" \
  "v_exp_f32_e32 v193, v109\n" \
  "v_mfma_f32_32x32x16_bf16 v[120:135], v[152:155], v[84:87], 0\n" \
  "v_exp_f32_e32 v194, v110\n" \
  "v_exp_f32_e32 v195, v111\n" \
  "v_pk_add_f32 v[188:189], v[188:189], 1.0 op_sel_hi:[1,0]\n" \
  "v_pk_add_f32 v[190:191], v[190:191], 1.0 op_sel_hi:[1,0]\n" \
  "v_rcp_f32_e32 v188, v188\n" \
  "v_mfma_f32_32x32x16_bf16 v[16:31], v[184:187], v[168:171], v[16:31]\n" \
  "v_rcp_f32_e32 v189, v189\n" \
  "s_waitcnt lgkmcnt(2)\n" \
  "v_lshrrev_b32_e32 v210, v103, v210\n" \
  "v_lshlrev_b32_sdwa v212, 4, v210 dst_sel:DWORD dst_unused:UNUSED_PAD src0_sel:DWORD src1_sel:BYTE_0\n" \
  "v_lshlrev_b32_sdwa v213, 4, v210 dst_sel:DWORD dst_unused:UNUSED_PAD src0_sel:DWORD src1_sel:BYTE_2\n" \
  "v_rcp_f32_e32 v190, v190\n" \
  "v_readfirstlane_b32 %[ps], %[tr]\n" \
  "v_rcp_f32_e32 v191, v191\n" \
  "ds_read_b128 v[164:167], v212 offset:%c[lst]\n" \
  "ds_read_b128 v[168:171], v213 offset:%c[lst]\n" \
  "v_pk_add_f32 v[192:193], v[192:193], 1.0 op_sel_hi:[1,0]\n" \
  "s_cmp_gt_i32 %[rem], %[ps]\n" \
  "s_cbranch_scc1 .Lh1_%=\n" \
  "s_setprio 0\n" \
  "s_branch .Lg1_%=\n" \
  ".Lh1_%=:\n" \
  "s_setprio 1\n" \
  ".Lg1_%=:\n" \
  "v_pk_add_f32 v[194:195], v[194:195], 1.0 op_sel_hi:[1,0]\n" \
  "v_mfma_f32_32x32x16_bf16 v[120:135], v[156:159], v[68:71], v[120:135]\n" \
  "v_exp_f32_e32 v196, v112\n" \
  "v_exp_f32_e32 v197, v113\n" \
  "v_exp_f32_e32 v198, v114\n" \
  "v_exp_f32_e32 v199, v115\n" \
  "v_rcp_f32_e32 v192, v192\n" \
  "v_rcp_f32_e32 v193, v193\n" \
  "v_mfma_f32_32x32x16_bf16 v[120:135], v[160:163], v[76:79], v[120:135]\n" \
  "v_rcp_f32_e32 v194, v194\n" \
  "s_waitcnt vmcnt(5)\n" \
  "s_cmp_gt_i32 %[rem], 2\n" \
  "s_cbranch_scc1 .Lw2_%=\n" \
  "s_waitcnt vmcnt(0)\n" \
  ".Lw2_%=:\n" \
  "ds_read_b32 v209, v95 offset:128\n" \
  "ds_read_b128 v[152:155], v101 offset:5120\n" \
  "v_rcp_f32_e32 v195, v195\n" \
  "v_pk_mul_f32 v[188:189], v[104:105], v[188:189]\n" \
  "v_pk_mul_f32 v[190:191], v[106:107], v[190:191]\n" \
  "v_pk_add_f32 v[196:197], v[196:197], 1.0 op_sel_hi:[1,0]\n" \
  "v_mfma_f32_32x32x16_bf16 v[120:135], v[136:139], v[36:39], v[120:135]\n" \
  "ds_read_b128 v[136:139], v97 offset:5120\n" \
  "v_pk_add_f32 v[198:199], v[198:199], 1.0 op_sel_hi:[1,0]\n" \
  "v_cvt_pk_bf16_f32 v172, v188, v189\n" \
  "v_cvt_pk_bf16_f32 v173, v190, v191\n" \
  "v_exp_f32_e32 v200, v116\n" \
  "s_waitcnt lgkmcnt(2)\n" \
  "v_and_b32_e32 v209, 0x7c, v209\n" \
  "v_lshl_add_u32 v211, v209, 2, v102\n" \
  "v_exp_f32_e32 v201, v117\n" \
  "ds_read_b128 v[156:159], v211 offset:256\n" \
  "ds_read_b128 v[160:163], v211\n" \
  "v_mfma_f32_32x32x16_bf16 v[120:135], v[140:143], v[44:47], v[120:135]\n" \
  "ds_read_b128 v[140:143], v98 offset:5120\n" \
  "v_exp_f32_e32 v202, v118\n" \
  "v_exp_f32_e32 v203, v119\n" \
  "v_rcp_f32_e32 v196, v196\n" \
  "v_rcp_f32_e32 v197, v197\n" \
  "v_rcp_f32_e32 v198, v198\n" \
  "v_rcp_f32_e32 v199, v199\n" \
  "v_mfma_f32_32x32x16_bf16 v[120:135], v[144:147], v[52:55], v[120:135]\n" \
  "ds_read_b128 v[144:147], v99 offset:5120\n" \
  "v_pk_mul_f32 v[192:193], v[108:109], v[192:193]\n" \
  "v_pk_mul_f32 v[194:195], v[110:111], v[194:195]\n" \
  "v_pk_add_f32 v[200:201], v[200:201], 1.0 op_sel_hi:[1,0]\n" \
  "v_pk_add_f32 v[202:203], v[202:203], 1.0 op_sel_hi:[1,0]\n" \
  "v_cvt_pk_bf16_f32 v174, v192, v193\n" \
  "v_cvt_pk_bf16_f32 v175, v194, v195\n" \
  "v_mfma_f32_32x32x16_bf16 v[120:135], v[148:151], v[60:63], v[120:135]\n" \
  "ds_read_b128 v[148:151], v100 offset:5120\n" \
  "v_rcp_f32_e32 v200, v200\n" \
  "v_rcp_f32_e32 v201, v201\n" \
  "v_rcp_f32_e32 v202, v202\n" \
  "v_rcp_f32_e32 v203, v203\n" \
  "v_pk_mul_f32 v[196:197], v[112:113], v[196:197]\n" \
  "v_pk_mul_f32 v[198:199], v[114:115], v[198:199]\n" \
  "v_cvt_pk_bf16_f32 v176, v196, v197\n" \
  "v_cvt_pk_bf16_f32 v177, v198, v199\n" \
  "v_pk_mul_f32 v[200:201], v[116:117], v[200:201]\n" \
  "v_pk_mul_f32 v[202:203], v[118:119], v[202:203]\n" \
  "v_cvt_pk_bf16_f32 v178, v200, v201\n" \
  "v_cvt_pk_bf16_f32 v179, v202, v203\n" \
  "v_exp_f32_e32 v188, v120\n" \
  "v_mfma_f32_32x32x16_bf16 v[0:15], v[172:175], v[164:167], v[0:15]\n" \
  "v_exp_f32_e32 v189, v121\n" \
  "v_exp_f32_e32 v190, v122\n" \
  "v_exp_f32_e32 v191, v123\n" \
  "v_exp_f32_e32 v192, v124\n" \
  "v_exp_f32_e32 v193, v125\n" \
  "s_waitcnt lgkmcnt(6)\n" \
  "v_mfma_f32_32x32x16_bf16 v[104:119], v[152:155], v[80:83], 0\n" \
  "v_exp_f32_e32 v194, v126\n" \
  "v_exp_f32_e32 v195, v127\n" \
  "v_pk_add_f32 v[188:189], v[188:189], 1.0 op_sel_hi:[1,0]\n" \
  "v_pk_add_f32 v[190:191], v[190:191], 1.0 op_sel_hi:[1,0]\n" \
  "v_rcp_f32_e32 v188, v188\n" \
  "v_mfma_f32_32x32x16_bf16 v[0:15], v[176:179], v[168:171], v[0:15]\n" \
  "v_rcp_f32_e32 v189, v189\n" \
  "v_rcp_f32_e32 v190, v190\n" \
  "v_rcp_f32_e32 v191, v191\n" \
  "v_pk_add_f32 v[192:193], v[192:193], 1.0 op_sel_hi:[1,0]\n" \
  "v_pk_add_f32 v[194:195], v[194:195], 1.0 op_sel_hi:[1,0]\n" \
  "s_waitcnt lgkmcnt(4)\n" \
  "v_mfma_f32_32x32x16_bf16 v[104:119], v[156:159], v[64:67], v[104:119]\n" \
  "v_exp_f32_e32 v196, v128\n" \
  "v_exp_f32_e32 v197, v129\n" \
  "v_exp_f32_e32 v198, v130\n" \
  "v_exp_f32_e32 v199, v131\n" \
  "v_rcp_f32_e32 v192, v192\n" \
  "v_rcp_f32_e32 v193, v193\n" \
  "s_waitcnt lgkmcnt(3)\n" \
  "v_mfma_f32_32x32x16_bf16 v[104:119], v[160:163], v[72:75], v[104:119]\n" \
  "v_rcp_f32_e32 v194, v194\n" \
  "s_waitcnt lgkmcnt(0)\n" \
  "v_and_or_b32 v204, v204, %[cmx], v88\n" \
  "v_and_or_b32 v205, v205, %[cmx], v89\n" \
  "v_and_or_b32 v206, v206, %[cmx], v90\n" \
  "v_and_or_b32 v207, v207, %[cmx], v91\n" \
  "v_or_b32_e32 v208, v208, v92\n" \
  "s_mov_b32 m0, %[mb1]\n" \
  "s_cmp_gt_i32 %[rem], 3\n" \
  "v_rcp_f32_e32 v195, v195\n" \
  "v_pk_mul_f32 v[188:189], v[120:121], v[188:189]\n" \
  "v_pk_mul_f32 v[190:191], v[122:123], v[190:191]\n" \
  "s_cbranch_scc0 .Ld3_%=\n" \
  "global_load_lds_dwordx4 v204, %[xb0] offset:0\n" \
  ".Ld3_%=:\n" \
  "v_pk_add_f32 v[196:197], v[196:197], 1.0 op_sel_hi:[1,0]\n" \
  "v_mfma_f32_32x32x16_bf16 v[104:119], v[136:139], v[32:35], v[104:119]\n" \
  "v_pk_add_f32 v[198:199], v[198:199], 1.0 op_sel_hi:[1,0]\n" \
  "v_cvt_pk_bf16_f32 v180, v188, v189\n" \
  "v_cvt_pk_bf16_f32 v181, v190, v191\n" \
  "s_cbranch_scc0 .Ld4_%=\n" \
  "global_load_lds_dwordx4 v205, %[xb1] offset:1024\n" \
  ".Ld4_%=:\n" \
  "v_exp_f32_e32 v200, v132\n" \
  "v_exp_f32_e32 v201, v133\n" \
  "v_mfma_f32_32x32x16_bf16 v[104:119], v[140:143], v[40:43], v[104:119]\n" \
  "v_exp_f32_e32 v202, v134\n" \
  "v_exp_f32_e32 v203, v135\n" \
  "v_rcp_f32_e32 v196, v196\n" \
  "s_cbranch_scc0 .Ld5_%=\n" \
  "global_load_lds_dwordx4 v206, %[xb2] offset:2048\n" \
  ".Ld5_%=:\n" \
  "v_rcp_f32_e32 v197, v197\n" \
  "v_rcp_f32_e32 v198, v198\n" \
  "v_rcp_f32_e32 v199, v199\n" \
  "v_mfma_f32_32x32x16_bf16 v[104:119], v[144:147], v[48:51], v[104:119]\n" \
  "v_pk_mul_f32 v[192:193], v[124:125], v[192:193]\n" \
  "v_pk_mul_f32 v[194:195], v[126:127], v[194:195]\n" \
  "v_pk_add_f32 v[200:201], v[200:201], 1.0 op_sel_hi:[1,0]\n" \
  "s_cbranch_scc0 .Ld6_%=\n" \
  "global_load_lds_dwordx4 v207, %[xb3] offset:3072\n" \
  ".Ld6_%=:\n" \
  "v_pk_add_f32 v[202:203], v[202:203], 1.0 op_sel_hi:[1,0]\n" \
  "v_cvt_pk_bf16_f32 v182, v192, v193\n" \
  "v_cvt_pk_bf16_f32 v183, v194, v195\n" \
  "v_mfma_f32_32x32x16_bf16 v[104:119], v[148:151], v[56:59], v[104:119]\n" \
  "v_rcp_f32_e32 v200, v200\n" \
  "v_rcp_f32_e32 v201, v201\n" \
  "v_rcp_f32_e32 v202, v202\n" \
  "v_rcp_f32_e32 v203, v203\n" \
  "s_mov_b32 m0, %[me1]\n" \
  "s_cbranch_scc0 .Ld7_%=\n" \
  "s_nop 0\n" \
  "global_load_lds_dwordx4 v208, %[eb] nt\n" \
  ".Ld7_%=:\n" \
  "v_pk_mul_f32 v[196:197], v[128:129], v[196:197]\n" \
  "v_pk_mul_f32 v[198:199], v[130:131], v[198:199]\n" \
  "v_cvt_pk_bf16_f32 v184, v196, v197\n" \
  "v_cvt_pk_bf16_f32 v185, v198, v199\n" \
  "v_pk_mul_f32 v[200:201], v[132:133], v[200:201]\n" \
  "v_pk_mul_f32 v[202:203], v[134:135], v[202:203]\n" \
  "v_cvt_pk_bf16_f32 v186, v200, v201\n" \
  "v_cvt_pk_bf16_f32 v187, v202, v203\n" \
  "s_sub_i32 %[rem], %[rem], 1\n" \
  "s_cmp_lt_i32 %[rem], 1\n" \
  "s_cbranch_scc1 .Ltail_%=\n" \
  "s_sub_i32 %[ps], %[rem], %[pb]\n" \
  "v_mov_b32_e32 %[tw], %[ps]\n" \
  "ds_write_b32 %[ao], %[tw]\n" \
  "ds_read_b32 %[tr], %[ap]\n" \
  "ds_read_b32 v210, v96 offset:128\n" \
  "ds_read_b128 v[204:207], v93 offset:512\n" \
  "ds_read_b32 v208, v94 offset:512\n" \
  "v_exp_f32_e32 v188, v104\n" \
  "v_mfma_f32_32x32x16_bf16 v[16:31], v[180:183], v[164:167], v[16:31]\n" \
  "v_exp_f32_e32 v189, v105\n" \
  "v_exp_f32_e32 v190, v106\n" \
  "v_exp_f32_e32 v191, v107\n" \
  "v_exp_f32_e32 v192, v108\n" \
  "v_exp_f32_e32 v193, v109\n" \
  "v_mfma_f32_32x32x16_bf16 v[120:135], v[152:155], v[84:87], 0\n" \
  "v_exp_f32_e32 v194, v110\n" \
  "v_exp_f32_e32 v195, v111\n" \
  "v_pk_add_f32 v[188:189], v[188:189], 1.0 op_sel_hi:[1,0]\n" \
  "v_pk_add_f32 v[190:191], v[190:191], 1.0 op_sel_hi:[1,0]\n" \
  "v_rcp_f32_e32 v188, v188\n" \
  "v_mfma_f32_32x32x16_bf16 v[16:31], v[184:187], v[168:171], v[16:31]\n" \
  "v_rcp_f32_e32 v189, v189\n" \
  "s_waitcnt lgkmcnt(2)\n" \
  "v_lshrrev_b32_e32 v210, v103, v210\n" \
  "v_lshlrev_b32_sdwa v212, 4, v210 dst_sel:DWORD dst_unused:UNUSED_PAD src0_sel:DWORD src1_sel:BYTE_0\n" \
  "v_lshlrev_b32_sdwa v213, 4, v210 dst_sel:DWORD dst_unused:UNUSED_PAD src0_sel:DWORD src1_sel:BYTE_2\n" \
  "v_rcp_f32_e32 v190, v190\n" \
  "v_readfirstlane_b32 %[ps], %[tr]\n" \
  "v_rcp_f32_e32 v191, v191\n" \
  "ds_read_b128 v[164:167], v212 offset:%c[lst]\n" \
  "ds_read_b128 v[168:171], v213 offset:%c[lst]\n" \
  "v_pk_add_f32 v[192:193], v[192:193], 1.0 op_sel_hi:[1,0]\n" \
  "s_cmp_gt_i32 %[rem], %[ps]\n" \
  "s_cbranch_scc1 .Lh8_%=\n" \
  "s_setprio 0\n" \
  "s_branch .Lg8_%=\n" \
  ".Lh8_%=:\n" \
  "s_setprio 1\n" \
  ".Lg8_%=:\n" \
  "v_pk_add_f32 v[194:195], v[194:195], 1.0 op_sel_hi:[1,0]\n" \
  "v_mfma_f32_32x32x16_bf16 v[120:135], v[156:159], v[68:71], v[120:135]\n" \
  "v_exp_f32_e32 v196, v112\n" \
  "v_exp_f32_e32 v197, v113\n" \
  "v_exp_f32_e32 v198, v114\n" \
  "v_exp_f32_e32 v199, v115\n" \
  "v_rcp_f32_e32 v192, v192\n" \
  "v_rcp_f32_e32 v193, v193\n" \
  "v_mfma_f32_32x32x16_bf16 v[120:135], v[160:163], v[76:79], v[120:135]\n" \
  "v_rcp_f32_e32 v194, v194\n" \
  "s_waitcnt vmcnt(5)\n" \
  "s_cmp_gt_i32 %[rem], 2\n" \
  "s_cbranch_scc1 .Lw9_%=\n" \
  "s_waitcnt vmcnt(0)\n" \
  ".Lw9_%=:\n" \
  "ds_read_b32 v209, v95 offset:256\n" \
  "ds_read_b128 v[152:155], v101 offset:0\n" \
  "v_rcp_f32_e32 v195, v195\n" \
  "v_pk_mul_f32 v[188:189], v[104:105], v[188:189]\n" \
  "v_pk_mul_f32 v[190:191], v[106:107], v[190:191]\n" \
  "v_pk_add_f32 v[196:197], v[196:197], 1.0 op_sel_hi:[1,0]\n" \
  "v_mfma_f32_32x32x16_bf16 v[120:135], v[136:139], v[36:39], v[120:135]\n" \
  "ds_read_b128 v[136:139], v97 offset:0\n" \
  "v_pk_add_f32 v[198:199], v[198:199], 1.0 op_sel_hi:[1,0]\n" \
  "v_cvt_pk_bf16_f32 v172, v188, v189\n" \
  "v_cvt_pk_bf16_f32 v173, v190, v191\n" \
  "v_exp_f32_e32 v200, v116\n" \
  "s_waitcnt lgkmcnt(2)\n" \
  "v_and_b32_e32 v209, 0x7c, v209\n" \
  "v_lshl_add_u32 v211, v209, 2, v102\n" \
  "v_exp_f32_e32 v201, v117\n" \
  "ds_read_b128 v[156:159], v211 offset:256\n" \
  "ds_read_b128 v[160:163], v211\n" \
  "v_mfma_f32_32x32x16_bf16 v[120:135], v[140:143], v[44:47], v[120:135]\n" \
  "ds_read_b128 v[140:143], v98 offset:0\n" \
  "v_exp_f32_e32 v202, v118\n" \
  "v_exp_f32_e32 v203, v119\n" \
  "v_rcp_f32_e32 v196, v196\n" \
  "v_rcp_f32_e32 v197, v197\n" \
  "v_rcp_f32_e32 v198, v198\n" \
  "v_rcp_f32_e32 v199, v199\n" \
  "v_mfma_f32_32x32x16_bf16 v[120:135], v[144:147], v[52:55], v[120:135]\n" \
  "ds_read_b128 v[144:147], v99 offset:0\n" \
  "v_pk_mul_f32 v[192:193], v[108:109], v[192:193]\n" \
  "v_pk_mul_f32 v[194:195], v[110:111], v[194:195]\n" \
  "v_pk_add_f32 v[200:201], v[200:201], 1.0 op_sel_hi:[1,0]\n" \
  "v_pk_add_f32 v[202:203], v[202:203], 1.0 op_sel_hi:[1,0]\n" \
  "v_cvt_pk_bf16_f32 v174, v192, v193\n" \
  "v_cvt_pk_bf16_f32 v175, v194, v195\n" \
  "v_mfma_f32_32x32x16_bf16 v[120:135], v[148:151], v[60:63], v[120:135]\n" \
  "ds_read_b128 v[148:151], v100 offset:0\n" \
  "v_rcp_f32_e32 v200, v200\n" \
  "v_rcp_f32_e32 v201, v201\n" \
  "v_rcp_f32_e32 v202, v202\n" \
  "v_rcp_f32_e32 v203, v203\n" \
  "v_pk_mul_f32 v[196:197], v[112:113], v[196:197]\n" \
  "v_pk_mul_f32 v[198:199], v[114:115], v[198:199]\n" \
  "v_cvt_pk_bf16_f32 v176, v196, v197\n" \
  "v_cvt_pk_bf16_f32 v177, v198, v199\n" \
  "v_pk_mul_f32 v[200:201], v[116:117], v[200:201]\n" \
  "v_pk_mul_f32 v[202:203], v[118:119], v[202:203]\n" \
  "v_cvt_pk_bf16_f32 v178, v200, v201\n" \
  "v_cvt_pk_bf16_f32 v179, v202, v203\n" \
  "v_exp_f32_e32 v188, v120\n" \
  "v_mfma_f32_32x32x16_bf16 v[0:15], v[172:175], v[164:167], v[0:15]\n" \
  "v_exp_f32_e32 v189, v121\n" \
  "v_exp_f32_e32 v190, v122\n" \
  "v_exp_f32_e32 v191, v123\n" \
  "v_exp_f32_e32 v192, v124\n" \
  "v_exp_f32_e32 v193, v125\n" \
  "s_waitcnt lgkmcnt(6)\n" \
  "v_mfma_f32_32x32x16_bf16 v[104:119], v[152:155], v[80:83], 0\n" \
  "v_exp_f32_e32 v194, v126\n" \
  "v_exp_f32_e32 v195, v127\n" \
  "v_pk_add_f32 v[188:189], v[188:189], 1.0 op_sel_hi:[1,0]\n" \
  "v_pk_add_f32 v[190:191], v[190:191], 1.0 op_sel_hi:[1,0]\n" \
  "v_rcp_f32_e32 v188, v188\n" \
  "v_mfma_f32_32x32x16_bf16 v[0:15], v[176:179], v[168:171], v[0:15]\n" \
  "v_rcp_f32_e32 v189, v189\n" \
  "v_rcp_f32_e32 v190, v190\n" \
  "v_rcp_f32_e32 v191, v191\n" \
  "v_pk_add_f32 v[192:193], v[192:193], 1.0 op_sel_hi:[1,0]\n" \
  "v_pk_add_f32 v[194:195], v[194:195], 1.0 op_sel_hi:[1,0]\n" \
  "s_waitcnt lgkmcnt(4)\n" \
  "v_mfma_f32_32x32x16_bf16 v[104:119], v[156:159], v[64:67], v[104:119]\n" \
  "v_exp_f32_e32 v196, v128\n" \
  "v_exp_f32_e32 v197, v129\n" \
  "v_exp_f32_e32 v198, v130\n" \
  "v_exp_f32_e32 v199, v131\n" \
  "v_rcp_f32_e32 v192, v192\n" \
  "v_rcp_f32_e32 v193, v193\n" \
  "s_waitcnt lgkmcnt(3)\n" \
  "v_mfma_f32_32x32x16_bf16 v[104:119], v[160:163], v[72:75], v[104:119]\n" \
  "v_rcp_f32_e32 v194, v194\n" \
  "s_waitcnt lgkmcnt(0)\n" \
  "v_and_or_b32 v204, v204, %[cmx], v88\n" \
  "v_and_or_b32 v205, v205, %[cmx], v89\n" \
  "v_and_or_b32 v206, v206, %[cmx], v90\n" \
  "v_and_or_b32 v207, v207, %[cmx], v91\n" \
  "v_or_b32_e32 v208, v208, v92\n" \
  "s_mov_b32 m0, %[mb0]\n" \
  "s_cmp_gt_i32 %[rem], 3\n" \
  "v_rcp_f32_e32 v195, v195\n" \
  "v_pk_mul_f32 v[188:189], v[120:121], v[188:189]\n" \
  "v_pk_mul_f32 v[190:191], v[122:123], v[190:191]\n" \
  "s_cbranch_scc0 .Ld10_%=\n" \
  "global_load_lds_dwordx4 v204, %[xb0] offset:0\n" \
  ".Ld10_%=:\n" \
  "v_pk_add_f32 v[196:197], v[196:197], 1.0 op_sel_hi:[1,0]\n" \
  "v_mfma_f32_32x32x16_bf16 v[104:119], v[136:139], v[32:35], v[104:119]\n" \
  "v_pk_add_f32 v[198:199], v[198:199], 1.0 op_sel_hi:[1,0]\n" \
  "v_cvt_pk_bf16_f32 v180, v188, v189\n" \
  "v_cvt_pk_bf16_f32 v181, v190, v191\n" \
  "s_cbranch_scc0 .Ld11_%=\n" \
  "global_load_lds_dwordx4 v205, %[xb1] offset:1024\n" \
  ".Ld11_%=:\n" \
  "v_exp_f32_e32 v200, v132\n" \
  "v_exp_f32_e32 v201, v133\n" \
  "v_mfma_f32_32x32x16_bf16 v[104:119], v[140:143], v[40:43], v[104:119]\n" \
  "v_exp_f32_e32 v202, v134\n" \
  "v_exp_f32_e32 v203, v135\n" \
  "v_rcp_f32_e32 v196, v196\n" \
  "s_cbranch_scc0 .Ld12_%=\n" \
  "global_load_lds_dwordx4 v206, %[xb2] offset:2048\n" \
  ".Ld12_%=:\n" \
  "v_rcp_f32_e32 v197, v197\n" \
  "v_rcp_f32_e32 v198, v198\n" \
  "v_rcp_f32_e32 v199, v199\n" \
  "v_mfma_f32_32x32x16_bf16 v[104:119], v[144:147], v[48:51], v[104:119]\n" \
  "v_pk_mul_f32 v[192:193], v[124:125], v[192:193]\n" \
  "v_pk_mul_f32 v[194:195], v[126:127], v[194:195]\n" \
  "v_pk_add_f32 v[200:201], v[200:201], 1.0 op_sel_hi:[1,0]\n" \
  "s_cbranch_scc0 .Ld13_%=\n" \
  "global_load_lds_dwordx4 v207, %[xb3] offset:3072\n" \
  ".Ld13_%=:\n" \
  "v_pk_add_f32 v[202:203], v[202:203], 1.0 op_sel_hi:[1,0]\n" \
  "v_cvt_pk_bf16_f32 v182, v192, v193\n" \
  "v_cvt_pk_bf16_f32 v183, v194, v195\n" \
  "v_mfma_f32_32x32x16_bf16 v[104:119], v[148:151], v[56:59], v[104:119]\n" \
  "v_rcp_f32_e32 v200, v200\n" \
  "v_rcp_f32_e32 v201, v201\n" \
  "v_rcp_f32_e32 v202, v202\n" \
  "v_rcp_f32_e32 v203, v203\n" \
  "s_mov_b32 m0, %[me0]\n" \
  "s_cbranch_scc0 .Ld14_%=\n" \
  "s_nop 0\n" \
  "global_load_lds_dwordx4 v208, %[eb] nt\n" \
  ".Ld14_%=:\n" \
  "v_pk_mul_f32 v[196:197], v[128:129], v[196:197]\n" \
  "v_pk_mul_f32 v[198:199], v[130:131], v[198:199]\n" \
  "v_cvt_pk_bf16_f32 v184, v196, v197\n" \
  "v_cvt_pk_bf16_f32 v185, v198, v199\n" \
  "v_pk_mul_f32 v[200:201], v[132:133], v[200:201]\n" \
  "v_pk_mul_f32 v[202:203], v[134:135], v[202:203]\n" \
  "v_cvt_pk_bf16_f32 v186, v200, v201\n" \
  "v_cvt_pk_bf16_f32 v187, v202, v203\n" \
  "v_add_u32_e32 v93, 256, v93\n" \
  "v_add_u32_e32 v94, 256, v94\n" \
  "v_add_u32_e32 v95, 256, v95\n" \
  "v_add_u32_e32 v96, 256, v96\n" \
  "s_sub_i32 %[rem], %[rem], 1\n" \
  "s_cmp_gt_i32 %[rem], 0\n" \
  "s_cbranch_scc1 .Lloop_%=\n" \
  ".Ltail_%=:\n" \
  "s_waitcnt lgkmcnt(0)\n" \
  "v_mfma_f32_32x32x16_bf16 v[16:31], v[180:183], v[164:167], v[16:31]\n" \
  "v_mfma_f32_32x32x16_bf16 v[16:31], v[184:187], v[168:171], v[16:31]\n" \
  "s_nop 7\n" \
  "s_nop 7\n" \
  ".Lend_%=:\n" \
  ""
#define K1T 512
#define STPOL "sc1"
constexpr int K1_STR = 272;
constexpr int K1_M = 0;
constexpr int K1_H = 256 * K1_STR;
constexpr int K1_END = K1_H + 1024 + 16;

static __device__ __forceinline__ void st16(char* p, u32x4 v) {
  asm volatile("global_store_dwordx4 %0, %1, off " STPOL "\n\ts_nop 1" ::"v"(p), "v"(v) : "memory");
}

constexpr int K1_J = (CH + K1T - 1) / K1T;
__global__ __launch_bounds__(K1T) void mpnn_k1(const int* __restrict__ src, const int* __restrict__ dst,
                                                const u16* __restrict__ wm, const u16* __restrict__ wu,
                                                char* __restrict__ ws) {
  __shared__ __attribute__((aligned(16))) char lds[K1_END];
  u32* hist = (u32*)(lds + K1_H);
  const int c = blockIdx.x, t = threadIdx.x, lane = t & 63;
  const int wv = __builtin_amdgcn_readfirstlane(t >> 6);
  const int ebase = c * CH;

  u32 dv[K1_J], sv[K1_J], pv[8];
  {
    const char* db = (const char*)(dst + ebase);
    const char* sb = (const char*)(src + ebase);
#pragma unroll
    for (int j = 0; j < K1_J; ++j) {
      const u32 o = 4u * min(t + K1T * j, CH - 1);
      asm volatile("global_load_dword %0, %1, %2" : "=&v"(dv[j]) : "v"(o), "s"(db) : "memory");
    }
#pragma unroll
    for (int j = 0; j < K1_J; ++j) {
      const u32 o = 4u * min(t + K1T * j, CH - 1);
      asm volatile("global_load_dword %0, %1, %2" : "=&v"(sv[j]) : "v"(o), "s"(sb) : "memory");
    }
  }
  const bool packs = wv == K1T / 64 - 2 && c < NFRAG;
  if (packs) {
    const int f = c, r = lane & 31, h = lane >> 5;
    const u16* pb = f < FR_WU ? wm : wu;
    const int g = f - FR_WU, T = g >> 3, ks = g & 7;
    int row0, col;
    if (f < FR_W3) { row0 = 64 + 16 * (f >> 1) + 8 * h; col = 32 * (f & 1) + r; }
    else if (f < FR_W1) { row0 = 128 + 8 * h; col = 32 * (f & 1) + r; }
    else if (f < FR_WU) { row0 = 16 * ((f - FR_W1) >> 1) + 8 * h; col = 32 * (f & 1) + r; }
    else { row0 = ks < 4 ? 16 * ks + 8 * h : 64 + 16 * (ks - 4) + 4 * h; col = phi(T, r); }
    const int hi_step = (f >= FR_WU && ks >= 4) ? 8 : 4;
    const u32 oa = (u32)(row0 * 64 + col) * 2u, ob = oa + (u32)hi_step * 128u;
    asm volatile(
        "global_load_ushort %0, %8, %10\n\t"
        "global_load_ushort %1, %8, %10 offset:128\n\t"
        "global_load_ushort %2, %8, %10 offset:256\n\t"
        "global_load_ushort %3, %8, %10 offset:384\n\t"
        "global_load_ushort %4, %9, %10\n\t"
        "global_load_ushort %5, %9, %10 offset:128\n\t"
        "global_load_ushort %6, %9, %10 offset:256\n\t"
        "global_load_ushort %7, %9, %10 offset:384"
        : "=&v"(pv[0]), "=&v"(pv[1]), "=&v"(pv[2]), "=&v"(pv[3]), "=&v"(pv[4]), "=&v"(pv[5]), "=&v"(pv[6]), "=&v"(pv[7])
        : "v"(oa), "v"(ob), "s"(pb)
        : "memory");
  }
  for (int i = t; i < 257; i += K1T) hist[i] = 0;
  LDS_BAR();
  WAIT_VM(0);
#pragma unroll
  for (int j = 0; j < K1_J; ++j) asm volatile("" : "+v"(dv[j]), "+v"(sv[j]));

  u32 bk[K1_J], rk[K1_J], rec[K1_J];
#pragma unroll
  for (int j = 0; j < K1_J; ++j) {
    const u32 v = win_of(dv[j]);
    bk[j] = v >> 3;
    rec[j] = (sv[j] << 7) | ((dv[j] - (u32)win_start((int)v)) << 2) | ((v & 7u) << 26);
    rk[j] = 0;
    if (j < K1_J - 1 || t + K1T * j < CH) rk[j] = atomicAdd(&hist[bk[j]], 1u);
  }
#pragma unroll
  for (int j = 0; j < K1_J; ++j) {
    const int i = t + K1T * j;
    if (j < K1_J - 1 || i < CH) {
      const u32 q5 = (u32)(ebase + i) << 5;
      if (rk[j] < 31) {
        *(u32x2*)(lds + K1_M + bk[j] * K1_STR + 8 + rk[j] * 8) = u32x2{rec[j], q5};
      } else {
        const u32 n = atomicAdd(&hist[256], 1u);
        *(u32x2*)(ws + WS_MO + (size_t)(ebase + n) * 8) = u32x2{rec[j], q5 | (bk[j] & 31u) | ((bk[j] >> 5) << 25)};
      }
    }
  }
  if (packs) {
    asm volatile("" : "+v"(pv[0]), "+v"(pv[1]), "+v"(pv[2]), "+v"(pv[3]), "+v"(pv[4]), "+v"(pv[5]), "+v"(pv[6]), "+v"(pv[7]));
    if (c >= FR_W3 && c < FR_WU) {
#pragma unroll
      for (int j = 0; j < 8; ++j) pv[j] = pk2(bf2f(pv[j]) * C_NLOG2E, 0.f) & 0xffffu;
    }
    const u32x4 p = {pv[0] | (pv[1] << 16), pv[2] | (pv[3] << 16), pv[4] | (pv[5] << 16), pv[6] | (pv[7] << 16)};
    *(u32x4*)(ws + WS_PK + (size_t)(c * 64 + lane) * 16) = p;
  }
  LDS_BAR();

  const u32 novf = hist[256];
#pragma unroll
  for (int j = 0; j < 2048 / K1T; ++j) {
    const int pc = t + K1T * j, bq = pc >> 3, s = pc & 7;
    const u32 cn = hist[bq];
    u32x4 ra = *(const u32x4*)(lds + K1_M + bq * K1_STR + s * 16);
    const u32x4 rb = *(const u32x4*)(lds + K1_M + bq * K1_STR + 128 + s * 16);
    if (s == 0) { ra[0] = cn | (novf << 12); ra[1] = 0; }
    const size_t o = ((size_t)bq * 256 + c) * 128 + s * 16;
    st16(ws + WS_MA + o, ra);
    if (cn > (u32)(15 + 2 * s)) st16(ws + WS_MB + o, rb);
  }
}

#define OPOL "sc0 sc1"
constexpr int L_ST = 0;
constexpr int L_TT = L_ST + 4096;
constexpr int L_CN = L_TT + 896;
constexpr int L_STG = L_TT + 1024;
constexpr int L_LS = L_STG + 81920;
constexpr int L_LQ = L_LS + 16384;
constexpr int L_MK = L_LQ + 16384;
constexpr int L_WU = L_MK + 16384;
constexpr int L_PR = L_WU + 16384;
constexpr int L_END = L_PR + 2048;

static __device__ __forceinline__ void list_rank(u32& pa, u32& pb, const u32x4& r, int ca, int cb, u32 one) {
  unsigned long long sv;
  u32 a;
  asm volatile(
      "s_mov_b64 %[sv], exec\n\t"
      "v_cmpx_lt_i32_e32 vcc, 0, %[ca]\n\t"
      "v_lshrrev_b32_e32 %[a], 24, %[ra]\n\t"
      "ds_add_rtn_u32 %[pa], %[a], %[one] offset:%c[cn]\n\t"
      "s_mov_b64 exec, %[sv]\n\t"
      "v_cmpx_lt_i32_e32 vcc, 0, %[cb]\n\t"
      "v_lshrrev_b32_e32 %[a], 24, %[rb]\n\t"
      "ds_add_rtn_u32 %[pb], %[a], %[one] offset:%c[cn]\n\t"
      "s_mov_b64 exec, %[sv]"
      : [pa] "=&v"(pa), [pb] "=&v"(pb), [a] "=&v"(a), [sv] "=&s"(sv)
      : [ra] "v"(r[0]), [rb] "v"(r[2]), [ca] "v"(ca), [cb] "v"(cb), [one] "v"(one), [cn] "i"(L_CN)
      : "vcc", "memory");
}
#define LIST_PUT1(P, R, Q)                                        \
  "v_min_u32_e32 " P ", %[cap], " P "\n\t"                        \
  "v_lshl_add_u32 %[a], " P ", 2, %[ls]\n\t"                      \
  "v_and_b32_e32 %[d], 0x7c, " R "\n\t"                           \
  "v_lshlrev_b32_e64 " P ", " P ", 1\n\t"                         \
  "v_and_or_b32 %[d], %[a], %[m], %[d]\n\t"                       \
  "ds_write2st64_b32 %[a], " R ", " Q " offset1:64\n\t"           \
  "ds_or_b32 %[d], " P " offset:32768\n\t"
static __device__ __forceinline__ void list_put(u32& pa, u32& pb, const u32x4& r, int ca, int cb) {
  unsigned long long sv;
  u32 a, d;
  asm volatile(
      "s_mov_b64 %[sv], exec\n\t"
      "v_cmpx_lt_i32_e32 vcc, 0, %[ca]\n\t"
      LIST_PUT1("%[pa]", "%[ra]", "%[qa]")
      "s_mov_b64 exec, %[sv]\n\t"
      "v_cmpx_lt_i32_e32 vcc, 0, %[cb]\n\t"
      LIST_PUT1("%[pb]", "%[rb]", "%[qb]")
      "s_mov_b64 exec, %[sv]"
      : [pa] "+v"(pa), [pb] "+v"(pb), [a] "=&v"(a), [d] "=&v"(d), [sv] "=&s"(sv)
      : [ra] "v"(r[0]), [qa] "v"(r[1]), [rb] "v"(r[2]), [qb] "v"(r[3]), [ca] "v"(ca), [cb] "v"(cb),
        [cap] "s"((u32)(8 * CAP - 1)), [ls] "s"((u32)L_LS), [m] "s"(~127u)
      : "vcc", "memory");
}
#define WAIT_LGKM0() asm volatile("s_waitcnt lgkmcnt(0)" ::: "memory")

__global__ __launch_bounds__(512, 2) void mpnn_k2(const u16* __restrict__ x, const u16* __restrict__ e,
                                                  char* __restrict__ ws, u16* __restrict__ out) {
  __shared__ __attribute__((aligned(16))) char lds[L_END + 256];
  const int t = threadIdx.x, lane = t & 63;
  const int w = __builtin_amdgcn_readfirstlane(t >> 6);
  const int b = (blockIdx.x & 7) * 32 + (blockIdx.x >> 3);
  const int v = 8 * b + w;
  const int ws0 = win_start(v), nwin = win_start(v + 1) - ws0;
  const int r = lane & 31, h = lane >> 5;
  const int pr = swap23(r);
  const u32 lbase = (u32)(size_t)lds;
  if (lbase != 0) return;

  const char* ma = ws + WS_MA + (size_t)b * 32768;
  const char* mb = ma + (WS_MB - WS_MA);
  const int node = ws0 + pr;
  u32x4 mr[4], xf[4];
  {
    const u32 o0 = 16u * t, o1 = o0 + 8192u, o2 = o0 + 16384u, o3 = o0 + 24576u;
    asm volatile(
        "global_load_dwordx4 %0, %4, %8\n\t"
        "global_load_dwordx4 %1, %5, %8\n\t"
        "global_load_dwordx4 %2, %6, %8\n\t"
        "global_load_dwordx4 %3, %7, %8"
        : "=&v"(mr[0]), "=&v"(mr[1]), "=&v"(mr[2]), "=&v"(mr[3])
        : "v"(o0), "v"(o1), "v"(o2), "v"(o3), "s"(ma)
        : "memory");
    const char* pk = ws + WS_PK;
#pragma unroll
    for (int k = 0; k < 5; ++k) {
      const int f = w + 8 * k;
      if (f < NFRAG) dma16((u32)(f * 1024 + lane * 16), pk, lbase + (f < FR_WU ? L_STG + f * 1024 : L_WU + (f - FR_WU) * 1024));
    }
    const u32 xo = (u32)min(node, NN - 1) * 128u + h * 16u;
    asm volatile(
        "global_load_dwordx4 %0, %4, %5\n\t"
        "global_load_dwordx4 %1, %4, %5 offset:32\n\t"
        "global_load_dwordx4 %2, %4, %5 offset:64\n\t"
        "global_load_dwordx4 %3, %4, %5 offset:96"
        : "=&v"(xf[0]), "=&v"(xf[1]), "=&v"(xf[2]), "=&v"(xf[3])
        : "v"(xo), "s"(x)
        : "memory");
  }

  const u32x4 z4 = {0, 0, 0, 0};
#pragma unroll
  for (int k = 0; k < 6; ++k) *(u32x4*)(lds + L_LS + (t + 512 * k) * 16) = z4;
  if (t < 128) *(u32x4*)(lds + L_PR + t * 16) = z4;
  if (t < 256) {
    u32x4 s;
#pragma unroll
    for (int i = 0; i < 4; ++i) s[i] = (((t >> (2 * i)) & 1) ? 0x3F80u : 0u) | (((t >> (2 * i + 1)) & 1) ? 0x3F800000u : 0u);
    *(u32x4*)(lds + L_ST + t * 16) = s;
  } else if (t < 256 + 56) {
    const int k = t - 256 - 24;
    u32x4 s;
#pragma unroll
    for (int i = 0; i < 4; ++i) s[i] = (k == 2 * i ? 0x3F80u : 0u) | (k == 2 * i + 1 ? 0x3F800000u : 0u);
    *(u32x4*)(lds + L_TT + (t - 256) * 16) = s;
  } else if (t >= 320 && t < 320 + 9) {
    *(u32*)(lds + L_CN + (t - 320) * 4) = t < 328 ? (u32)(t - 320) * CAP : 0u;
  }
  WAIT_VM(4);
  asm volatile("" : "+v"(mr[0]), "+v"(mr[1]), "+v"(mr[2]), "+v"(mr[3]));
  const int s8 = t & 7;
  u32 hdr[4];
#pragma unroll
  for (int k = 0; k < 4; ++k) {
    const int q = __builtin_amdgcn_update_dpp(0, (int)mr[k][0], 0x00, 0xf, 0xf, false);
    hdr[k] = (u32)__builtin_amdgcn_update_dpp(q, q, 0x114, 0xf, 0xa, false);
  }
  const u32 hB = s8 & 4 ? (s8 & 2 ? hdr[3] : hdr[2]) : (s8 & 2 ? hdr[1] : hdr[0]);
  const int clB = (int)(hB & 0xfffu) - (15 + 2 * (s8 & 1));
  const u32 oB = 128u * ((t >> 3) + 64 * (s8 >> 1)) + 16u * (s8 & 1);
  u32x4 rB0 = {0, 0, 0, 0}, rB1 = {0, 0, 0, 0}, rB2 = {0, 0, 0, 0}, rB3 = {0, 0, 0, 0};
  if (clB > 0) asm volatile("global_load_dwordx4 %0, %1, %2" : "+v"(rB0) : "v"(oB), "s"(mb) : "memory");
  if (clB > 4) asm volatile("global_load_dwordx4 %0, %1, %2 offset:32" : "+v"(rB1) : "v"(oB), "s"(mb) : "memory");
  if (clB > 8) asm volatile("global_load_dwordx4 %0, %1, %2 offset:64" : "+v"(rB2) : "v"(oB), "s"(mb) : "memory");
  if (clB > 12) asm volatile("global_load_dwordx4 %0, %1, %2 offset:96" : "+v"(rB3) : "v"(oB), "s"(mb) : "memory");
  {
    const int c0 = (int)(hdr[0] & 0xfffu) - 2 * s8;
    const bool ok = s8 == 0 ? c0 > 0 : c0 + 1 > 0;
    const u32 eo = ok ? (s8 == 0 ? mr[0][3] : mr[0][1]) & 0x01ffffe0u : 0u;
    asm volatile("s_mov_b32 m0, %1\n\ts_nop 0\n\tglobal_load_lds_dword %0, %2" ::"v"(eo), "s"(lbase + L_END), "s"(e) : "memory");
  }
  LDS_BAR();

  u32* cnt = (u32*)(lds + L_CN);
  u32* LS = (u32*)(lds + L_LS);
  u32* LQ = (u32*)(lds + L_LQ);
  u32* MK = (u32*)(lds + L_MK);

  {
    u32 P[8];
    int ca[4], cb[4];
    const u32 one = 1;
#pragma unroll
    for (int k = 0; k < 4; ++k) {
      cb[k] = (int)(hdr[k] & 0xfffu) - 2 * s8;
      ca[k] = s8 == 0 ? 0 : cb[k] + 1;
      list_rank(P[2 * k], P[2 * k + 1], mr[k], ca[k], cb[k], one);
    }
    WAIT_LGKM0();
#pragma unroll
    for (int k = 0; k < 4; ++k) list_put(P[2 * k], P[2 * k + 1], mr[k], ca[k], cb[k]);
    WAIT_VM(1);
    asm volatile("" : "+v"(rB0), "+v"(rB1), "+v"(rB2), "+v"(rB3));
    list_rank(P[0], P[1], rB0, clB, clB - 1, one);
    list_rank(P[2], P[3], rB1, clB - 4, clB - 5, one);
    WAIT_LGKM0();
    list_put(P[0], P[1], rB0, clB, clB - 1);
    list_put(P[2], P[3], rB1, clB - 4, clB - 5);
    if (__builtin_amdgcn_ballot_w64(clB > 8) != 0) {
      list_rank(P[0], P[1], rB2, clB - 8, clB - 9, one);
      list_rank(P[2], P[3], rB3, clB - 12, clB - 13, one);
      WAIT_LGKM0();
      list_put(P[0], P[1], rB2, clB - 8, clB - 9);
      list_put(P[2], P[3], rB3, clB - 12, clB - 13);
      const int count = hB & 0xfff, novf = hB >> 12;
      if (count > 31) {
        const int cc = (t >> 3) + 64 * (s8 >> 1);
        if (novf > 128) cnt[8] = 1;
        else
          for (int i = s8 & 1; i < novf; i += 2) {
            const u32x2 en = *(const u32x2*)(ws + WS_MO + (size_t)(cc * CH + i) * 8);
            if (((en[1] & 31u) | ((en[1] >> 25) << 5)) == (u32)b) {
              const u32 p = min(atomicAdd((u32*)((char*)cnt + (en[0] >> 24)), 1u), (u32)(8 * CAP - 1));
              LS[p] = en[0];
              LQ[p] = en[1] & 0x01ffffe0u;
              atomicOr(&MK[(p & ~31u) | ((en[0] >> 2) & 31u)], 1u << (p & 31u));
            }
          }
      }
    }
  }

  u32x4 w1[8], w3[2], u2[2][2];
#pragma unroll
  for (int f = 0; f < 8; ++f) w1[f] = *(const u32x4*)(lds + L_STG + (FR_W1 + f) * 1024 + lane * 16);
#pragma unroll
  for (int f = 0; f < 2; ++f) w3[f] = *(const u32x4*)(lds + L_STG + (FR_W3 + f) * 1024 + lane * 16);
  {
    asm volatile("" : "+v"(xf[0]), "+v"(xf[1]), "+v"(xf[2]), "+v"(xf[3]));
    f32x16 a0 = {0}, a1 = {0};
#pragma unroll
    for (int s = 0; s < 4; ++s) {
      a0 = mfma(xf[s], *(const u32x4*)(lds + L_STG + (FR_W2 + 2 * s) * 1024 + lane * 16), a0);
      a1 = mfma(xf[s], *(const u32x4*)(lds + L_STG + (FR_W2 + 2 * s + 1) * 1024 + lane * 16), a1);
    }
#pragma unroll
    for (int s = 0; s < 2; ++s)
#pragma unroll
      for (int k = 0; k < 4; ++k) {
        u2[s][0][k] = pk2(a0[8 * s + 2 * k] * C_NLOG2E, a0[8 * s + 2 * k + 1] * C_NLOG2E);
        u2[s][1][k] = pk2(a1[8 * s + 2 * k] * C_NLOG2E, a1[8 * s + 2 * k + 1] * C_NLOG2E);
      }
  }
  LDS_BAR();

  bool slow;
  {
    const u32x4 c0 = *(const u32x4*)(cnt), c1 = *(const u32x4*)(cnt + 4);
    u32 over = cnt[8];
#pragma unroll
    for (int k = 0; k < 4; ++k) over |= (c0[k] - CAP * k > CAP) | (c1[k] - CAP * (4 + k) > CAP);
    slow = __builtin_amdgcn_readfirstlane(over) != 0;
  }
  int sc = 0, si = 0;

  const u32 stg = L_STG + w * 10240;
  const char* xbase = (const char*)x;
  const char* esbase = (const char*)e;
  f32x16 ag0 = {0}, ag1 = {0};
  u32x32 w1v;
  u32x16 u2v;
  u32x8 w3v;
#pragma unroll
  for (int k = 0; k < 4; ++k) {
#pragma unroll
    for (int f = 0; f < 8; ++f) w1v[4 * f + k] = w1[f][k];
#pragma unroll
    for (int f = 0; f < 4; ++f) u2v[4 * f + k] = u2[f >> 1][f & 1][k];
#pragma unroll
    for (int f = 0; f < 2; ++f) w3v[4 * f + k] = w3[f][k];
  }

  for (;;) {
    if (slow) {
      __syncthreads();
#pragma unroll
      for (int k = 0; k < 6; ++k) *(u32x4*)(lds + L_LS + (t + 512 * k) * 16) = z4;
      if (t < 8) cnt[t] = t * CAP;
      __syncthreads();
      const u32 hd = *(const u32*)(ma + (size_t)sc * 128);
      const int count = hd & 0xfff;
      const int items = count > 31 ? 31 + (int)(hd >> 12) : count;
      const int i = si + t;
      if (i < items) {
        u32x2 en = i < 15   ? *(const u32x2*)(ma + (size_t)sc * 128 + 8 + i * 8)
                   : i < 31 ? *(const u32x2*)(mb + (size_t)sc * 128 + (i - 15) * 8)
                            : *(const u32x2*)(ws + WS_MO + (size_t)(sc * CH + i - 31) * 8);
        if (i < 31 || ((en[1] & 31u) | ((en[1] >> 25) << 5)) == (u32)b) {
          const u32 p = atomicAdd((u32*)((char*)cnt + (en[0] >> 24)), 1u);
          LS[p] = en[0];
          LQ[p] = en[1] & 0x01ffffe0u;
          atomicOr(&MK[(p & ~31u) | ((en[0] >> 2) & 31u)], 1u << (p & 31u));
        }
      }
      si += CAP;
      if (si >= __builtin_amdgcn_readfirstlane(items)) { si = 0; ++sc; }
      __syncthreads();
    }
    const int ng = (__builtin_amdgcn_readfirstlane(cnt[w]) - w * CAP + 31) >> 5;
    {
      int ln = lane;
      asm volatile("" : "+v"(ln));
      const int r = ln & 31, h = ln >> 5, pr = swap23(r), o8 = ln >> 3;
      u32x16 cv;
#pragma unroll
      for (int i = 0; i < 4; ++i) cv[i] = (u32)(((ln & 7) ^ (2 * i + (o8 >> 2))) << 4);
      cv[4] = (u32)((((ln & 1) ^ ((ln >> 4) & 1))) << 4);
      cv[5] = lbase + L_LS + w * (CAP * 4) + o8 * 16;
      cv[6] = lbase + L_LQ + w * (CAP * 4) + swap23(ln >> 1) * 4;
      cv[7] = lbase + L_LS + w * (CAP * 4) + pr * 4;
      cv[8] = lbase + L_MK + w * 2048 + pr * 4;
      {
        const int i = pr & 3, o = pr >> 2;
        const int a_off = stg + (8 * i + o) * 128 + ((h ^ (2 * i + (o >> 2))) << 4);
#pragma unroll
        for (int s = 0; s < 4; ++s) cv[9 + s] = lbase + (a_off ^ (s << 5));
      }
      cv[13] = lbase + stg + 4096 + r * 32 + ((h ^ ((r >> 3) & 1)) << 4);
      cv[14] = lbase + L_TT + (8 - 8 * h) * 16;
      cv[15] = 8 * h;
      int rem = ng;
      const u32 mb0 = lbase + stg;
      const u32 pao = lbase + L_PR + w * 256 + ln * 4, pap = lbase + L_PR + (w ^ 4) * 256;
      const u32 ppb = (u32)((w >> 2) ^ 1);
      u32 ptw, ptr, pps;
      asm volatile(K2_LOOP_ASM
                   : "+{v[0:15]}"(ag0), "+{v[16:31]}"(ag1), "+{v[88:103]}"(cv), [rem] "+s"(rem),
                     [tw] "=&v"(ptw), [tr] "=&v"(ptr), [ps] "=&s"(pps)
                   : "{v[32:63]}"(w1v), "{v[64:79]}"(u2v), "{v[80:87]}"(w3v), [mb0] "s"(mb0), [mb1] "s"(mb0 + 5120),
                     [me0] "s"(mb0 + 4096), [me1] "s"(mb0 + 9216), [xb0] "s"(xbase), [xb1] "s"(xbase - 1024), [xb2] "s"(xbase - 2048),
                     [xb3] "s"(xbase - 3072), [eb] "s"(esbase),
                     [cmx] "s"(0x7FFF80u), [lst] "i"(L_ST), [ao] "v"(pao), [ap] "v"(pap), [pb] "s"(ppb)
                   : "scc", "memory", CLOB);
    }
    if (!slow || sc >= 256) break;
  }

  WAIT_VM(0);

  u32x4 gf[4];
#pragma unroll
  for (int s = 0; s < 2; ++s)
#pragma unroll
    for (int k = 0; k < 4; ++k) {
      gf[s][k] = pk2(ag0[8 * s + 2 * k] * C_NLN2, ag0[8 * s + 2 * k + 1] * C_NLN2);
      gf[2 + s][k] = pk2(ag1[8 * s + 2 * k] * C_NLN2, ag1[8 * s + 2 * k + 1] * C_NLN2);
    }
  f32x16 o0 = {0}, o1 = {0};
#pragma unroll
  for (int ks = 0; ks < 4; ++ks) {
    o0 = mfma(*(const u32x4*)(lds + L_WU + ks * 1024 + lane * 16), xf[ks], o0);
    o1 = mfma(*(const u32x4*)(lds + L_WU + (8 + ks) * 1024 + lane * 16), xf[ks], o1);
  }
#pragma unroll
  for (int ks = 0; ks < 4; ++ks) {
    o0 = mfma(*(const u32x4*)(lds + L_WU + (4 + ks) * 1024 + lane * 16), gf[ks], o0);
    o1 = mfma(*(const u32x4*)(lds + L_WU + (12 + ks) * 1024 + lane * 16), gf[ks], o1);
  }
  {
    char* tile = lds + stg;
#pragma unroll
    for (int q = 0; q < 2; ++q) {
      u32x4 s0, s1;
#pragma unroll
      for (int k = 0; k < 4; ++k) {
        float a = o0[8 * q + 2 * k], bb = o0[8 * q + 2 * k + 1];
        s0[k] = pk2(a * __builtin_amdgcn_rcpf(1.f + __builtin_amdgcn_exp2f(a * C_NLOG2E)),
                    bb * __builtin_amdgcn_rcpf(1.f + __builtin_amdgcn_exp2f(bb * C_NLOG2E)));
        a = o1[8 * q + 2 * k]; bb = o1[8 * q + 2 * k + 1];
        s1[k] = pk2(a * __builtin_amdgcn_rcpf(1.f + __builtin_amdgcn_exp2f(a * C_NLOG2E)),
                    bb * __builtin_amdgcn_rcpf(1.f + __builtin_amdgcn_exp2f(bb * C_NLOG2E)));
      }
      *(u32x4*)(tile + pr * 128 + (((4 * h + q) ^ (pr & 7)) << 4)) = s0;
      *(u32x4*)(tile + pr * 128 + (((4 * h + 2 + q) ^ (pr & 7)) << 4)) = s1;
    }
    const int o8 = lane >> 3, p8 = lane & 7;
    char* op = (char*)out + (size_t)(ws0 + o8) * 128 + p8 * 16;
#pragma unroll
    for (int i = 0; i < 4; ++i) {
      const u32x4 s = *(const u32x4*)(tile + (8 * i + o8) * 128 + ((p8 ^ o8) << 4));
      if (i < 3 || 24 + o8 < nwin)
        asm volatile("global_store_dwordx4 %0, %1, off offset:%2 " OPOL "\n\ts_nop 1" ::"v"(op), "v"(s), "n"(1024 * i) : "memory");
    }
  }
}

extern "C" void kernel_launch(void* const* d_in, const int* in_sizes, int n_in, void* d_out, int out_size,
                              void* d_ws, size_t ws_size, hipStream_t stream) {
  const u16* x = (const u16*)d_in[0];
  const u16* e = (const u16*)d_in[1];
  const int* src = (const int*)d_in[2];
  const int* dst = (const int*)d_in[3];
  const u16* wm = (const u16*)d_in[4];
  const u16* wu = (const u16*)d_in[5];
  mpnn_k1<<<256, K1T, 0, stream>>>(src, dst, wm, wu, (char*)d_ws);
  mpnn_k2<<<256, 512, 0, stream>>>(x, e, (char*)d_ws, (u16*)d_out);
}
```
